```python
import torch

def _softmax(x, axis=-1):
    return torch.softmax(x, dim=axis)
def _gelu(x):
    return torch.nn.functional.gelu(x, approximate="tanh")
def _silu(x):
    return torch.nn.functional.silu(x)
def _sigmoid(x):
    return torch.sigmoid(x)
def _layernorm(x, axis=-1, eps=1e-5):
    m = x.mean(dim=axis, keepdim=True)
    v = x.var(dim=axis, keepdim=True, unbiased=False)
    return (x - m) / torch.sqrt(v + eps)
def _scatter_add(tgt, idx, src, axis=0):
    return tgt.clone().index_add_(axis, idx, src)
def _segment_sum(src, seg, n):
    out = torch.zeros((n,) + src.shape[1:], dtype=src.dtype)
    return out.index_add_(0, seg, src)
def _pad(x, pw):
    flat = tuple(v for pair in reversed(pw) for v in pair)
    return torch.nn.functional.pad(x, flat)

kernel_name = 'standalone__sequential_behavior_din__recsys'

def setup_inputs():

    _w = (lambda a: (lambda t: t.to(torch.bfloat16) if t.is_floating_point() else t)(torch.as_tensor(a)))
    B, L, D = 16384, 50, 16
    H = D
    return {
        "target": _w((lambda *s: torch.randn(*s, dtype=torch.bfloat16) * 1.6)(B, D)),
        "hist": _w((lambda *s: torch.randn(*s, dtype=torch.bfloat16) * 1.6)(B, L, D)),
        "mask": _w((lambda lo, hi, *s: torch.randint(lo, hi, s))(0, 2, B, L).to(float)),
        "w1": _w((lambda *s: torch.randn(*s, dtype=torch.bfloat16) * 1.6)(4 * D, H) * (4 * D) ** -0.5),
        "b1": _w((lambda *s: torch.randn(*s, dtype=torch.bfloat16) * 1.6)(H) * 0.1),
        "w2": _w((lambda *s: torch.randn(*s, dtype=torch.bfloat16) * 0.05)(H, 1) * H ** -0.5),
        "b2": _w((lambda *s: torch.randn(*s, dtype=torch.bfloat16) * 0.05)(1) * 0.1),
    }

def reference(target, hist, mask, w1, b1, w2, b2):
    L = hist.shape[1]
    tgt = (lambda x, n, axis: torch.repeat_interleave(x, n, dim=axis))(target[:, None, :], L, axis=1)
    feat = torch.concatenate([hist, tgt, hist - tgt, hist * tgt], axis=-1)
    h = _sigmoid(feat @ w1 + b1)
    scores = (h @ w2 + b2)[..., 0] * mask
    return torch.einsum('bl,bld->bd', scores, hist)
```

```cpp
#include <hip/hip_runtime.h>
#include <hip/hip_bf16.h>

__device__ __forceinline__ float bf2f(unsigned short u) { return __uint_as_float(((unsigned)u) << 16); }

__global__ __launch_bounds__(256) void din_naive(const unsigned short* __restrict__ target,
                                                 const unsigned short* __restrict__ hist,
                                                 const unsigned short* __restrict__ mask,
                                                 const unsigned short* __restrict__ w1,
                                                 const unsigned short* __restrict__ b1,
                                                 const unsigned short* __restrict__ w2,
                                                 const unsigned short* __restrict__ b2,
                                                 __hip_bfloat16* __restrict__ out, int B) {
  __shared__ float sw1[64 * 16];
  __shared__ float sb1[16], sw2[16];
  for (int i = threadIdx.x; i < 1024; i += 256) sw1[i] = bf2f(w1[i]);
  if (threadIdx.x < 16) { sb1[threadIdx.x] = bf2f(b1[threadIdx.x]); sw2[threadIdx.x] = bf2f(w2[threadIdx.x]); }
  __syncthreads();
  int lane = threadIdx.x & 63;
  int b = blockIdx.x * 4 + (threadIdx.x >> 6);
  if (b >= B) return;
  float t[16], h[16];
  for (int d = 0; d < 16; ++d) t[d] = bf2f(target[b * 16 + d]);
  float score = 0.f;
  if (lane < 50) {
    for (int d = 0; d < 16; ++d) h[d] = bf2f(hist[(size_t)(b * 50 + lane) * 16 + d]);
    float s = bf2f(b2[0]);
    for (int j = 0; j < 16; ++j) {
      float p = sb1[j];
      for (int d = 0; d < 16; ++d) {
        p += h[d] * sw1[d * 16 + j];
        p += t[d] * sw1[(16 + d) * 16 + j];
        p += (h[d] - t[d]) * sw1[(32 + d) * 16 + j];
        p += (h[d] * t[d]) * sw1[(48 + d) * 16 + j];
      }
      float sg = 1.f / (1.f + __expf(-p));
      s += sg * sw2[j];
    }
    score = s * bf2f(mask[b * 50 + lane]);
  } else {
    for (int d = 0; d < 16; ++d) h[d] = 0.f;
  }
  for (int d = 0; d < 16; ++d) {
    float v = score * h[d];
    for (int o = 32; o > 0; o >>= 1) v += __shfl_xor(v, o, 64);
    if (lane == d) out[b * 16 + d] = __float2bfloat16(v);
  }
}

extern "C" void kernel_launch(void* const* d_in, const int* in_sizes, int n_in,
                              void* d_out, int out_size, void* d_ws, size_t ws_size,
                              hipStream_t stream) {
  int B = in_sizes[0] / 16;
  din_naive<<<(B + 3) / 4, 256, 0, stream>>>((const unsigned short*)d_in[0], (const unsigned short*)d_in[1],
                                             (const unsigned short*)d_in[2], (const unsigned short*)d_in[3],
                                             (const unsigned short*)d_in[4], (const unsigned short*)d_in[5],
                                             (const unsigned short*)d_in[6], (__hip_bfloat16*)d_out, B);
}
```

```cpp
#include <hip/hip_runtime.h>
#include <hip/hip_bf16.h>

typedef float f32x4 __attribute__((ext_vector_type(4)));
typedef float f32x2 __attribute__((ext_vector_type(2)));
typedef short s16x4 __attribute__((ext_vector_type(4)));
typedef __bf16 bf16x8 __attribute__((ext_vector_type(8)));
typedef __bf16 bf16x2 __attribute__((ext_vector_type(2)));
typedef unsigned u32x2 __attribute__((ext_vector_type(2)));
typedef unsigned u32x4 __attribute__((ext_vector_type(4)));
typedef u32x2 __attribute__((may_alias)) u32x2a;
typedef u32x4 __attribute__((may_alias)) u32x4a;
typedef unsigned short __attribute__((may_alias)) u16a;
#define LAS __attribute__((address_space(3)))

#define WAVES 4
#define NT " nt"

constexpr int O_MASK = 6400;
constexpr int O_TGT = 6800;
constexpr int O_B1 = 6928;
constexpr int O_W2 = 6960;
constexpr int O_SC = 6992;
constexpr int SC_ROW = 448;
constexpr int O_DUMMY = O_SC + 4 * SC_ROW;
constexpr int WREG = O_DUMMY + 16;
constexpr int O_W1 = WAVES * WREG;
constexpr int LDS_BYTES = O_W1 + 2048;

__device__ __forceinline__ unsigned cvtpk(float lo, float hi) {
  f32x2 v = {lo, hi};
  bf16x2 b = __builtin_convertvector(v, bf16x2);
  return __builtin_bit_cast(unsigned, b);
}
__device__ __forceinline__ float bflo(unsigned u) { return __uint_as_float(u << 16); }
__device__ __forceinline__ float bfhi(unsigned u) { return __uint_as_float(u & 0xffff0000u); }
__device__ __forceinline__ u32x2 lds_rd64(unsigned a) { return *(const LAS u32x2a*)(a); }
__device__ __forceinline__ u32x4 lds_rd128(unsigned a) { return *(const LAS u32x4a*)(a); }
__device__ __forceinline__ u32x2 lds_tr(unsigned a) {
  return __builtin_bit_cast(u32x2, __builtin_amdgcn_ds_read_tr16_b64_v4i16((LAS s16x4*)(a)));
}
__device__ __forceinline__ f32x4 mfma16(u32x2 a, u32x2 b, f32x4 c) {
  return __builtin_amdgcn_mfma_f32_16x16x16bf16_1k(__builtin_bit_cast(s16x4, a), __builtin_bit_cast(s16x4, b), c, 0, 0, 0);
}
__device__ __forceinline__ f32x4 mfma32(u32x4 a, u32x4 b, f32x4 c) {
  return __builtin_amdgcn_mfma_f32_16x16x32_bf16(__builtin_bit_cast(bf16x8, a), __builtin_bit_cast(bf16x8, b), c, 0, 0, 0);
}
__device__ __forceinline__ u32x2 sig4(f32x4 y) {
  float s0 = __builtin_amdgcn_rcpf(1.0f + __builtin_amdgcn_exp2f(y[0]));
  float s1 = __builtin_amdgcn_rcpf(1.0f + __builtin_amdgcn_exp2f(y[1]));
  float s2 = __builtin_amdgcn_rcpf(1.0f + __builtin_amdgcn_exp2f(y[2]));
  float s3 = __builtin_amdgcn_rcpf(1.0f + __builtin_amdgcn_exp2f(y[3]));
  u32x2 r = {cvtpk(s0, s1), cvtpk(s2, s3)};
  return r;
}
#define VMWAIT(N)                                          \
  do {                                                     \
    __builtin_amdgcn_sched_barrier(0);                     \
    asm volatile("s_waitcnt vmcnt(" #N ")" ::: "memory"); \
  } while (0)

__global__ __launch_bounds__(WAVES * 64, 4) void din_kernel(const char* __restrict__ target, const char* __restrict__ hist,
                                                         const char* __restrict__ mask, const char* __restrict__ w1,
                                                         const char* __restrict__ b1, const char* __restrict__ w2,
                                                         const char* __restrict__ b2, char* __restrict__ out) {
  __shared__ __attribute__((aligned(16))) char smem[LDS_BYTES];
  const unsigned lane = threadIdx.x & 63;
  const unsigned w = __builtin_amdgcn_readfirstlane(threadIdx.x >> 6);
  const unsigned gw = blockIdx.x * WAVES + w;
  const unsigned sbase = (unsigned)(size_t)(LAS char*)smem;
  const unsigned reg = sbase + w * WREG;
  const unsigned n = lane & 15, g = lane >> 4;

  unsigned b2raw;
  {
    const char* hb = hist + (size_t)gw * 6400;
    const unsigned long long p0 = (unsigned long long)hb + 6144;
    const unsigned long long p1 = (unsigned long long)mask + (size_t)gw * 400 - 16 * 16;
    const unsigned long long p2 = (unsigned long long)target + (size_t)gw * 128 - 41 * 16;
    const unsigned long long p3 = (unsigned long long)b1 - 49 * 16;
    const unsigned long long p4 = (unsigned long long)w2 - 51 * 16;
    unsigned long long src = lane < 16 ? p0 : p1;
    src = lane < 41 ? src : p2;
    src = lane < 49 ? src : p3;
    src = lane < 51 ? src : p4;
    src += (lane < 52 ? lane : 52u) * 16;
    const unsigned voff = lane * 16;
    const unsigned wl = (2048 / WAVES) / 16;
    const unsigned woff = w * (2048 / WAVES) + (lane & (wl - 1)) * 16;
    unsigned keep;
    unsigned long long ex;
    asm volatile(
        "s_mov_b32 %[keep], m0\n\t"
        "s_mov_b64 %[ex], exec\n\t"
        "global_load_ushort %[b2v], %[zero], %[b2p]\n\t"
        "s_mov_b32 m0, %[lw]\n\t"
        "s_mov_b64 exec, %[exw]\n\t"
        "global_load_lds_dwordx4 %[woff], %[w1p]\n\t"
        "s_mov_b32 m0, %[lc]\n\t"
        "s_mov_b64 exec, %[exc]\n\t"
        "global_load_lds_dwordx4 %[src], off" NT "\n\t"
        "s_mov_b32 m0, %[l0]\n\t"
        "s_mov_b64 exec, %[ex]\n\t"
        "global_load_lds_dwordx4 %[voff], %[hb]" NT "\n\t"
        "global_load_lds_dwordx4 %[voff], %[hb] offset:1024" NT "\n\t"
        "global_load_lds_dwordx4 %[voff], %[hb] offset:2048" NT "\n\t"
        "global_load_lds_dwordx4 %[voff], %[hb] offset:3072" NT "\n\t"
        "s_mov_b32 m0, %[l1]\n\t"
        "s_nop 0\n\t"
        "global_load_lds_dwordx4 %[voff], %[hb2]" NT "\n\t"
        "global_load_lds_dwordx4 %[voff], %[hb2] offset:1024" NT "\n\t"
        "s_mov_b32 m0, %[keep]"
        : [keep] "=&s"(keep), [ex] "=&s"(ex), [b2v] "=&v"(b2raw)
        : [zero] "v"(0u), [b2p] "s"(b2), [lw] "s"(sbase + O_W1 + w * (2048 / WAVES)),
          [exw] "s"(wl >= 64 ? ~0ull : ((1ull << (wl & 63)) - 1)), [woff] "v"(woff), [w1p] "s"(w1), [lc] "s"(reg + 6144),
          [exc] "s"((1ull << 53) - 1), [src] "v"(src), [l0] "s"(reg), [voff] "v"(voff), [hb] "s"(hb),
          [l1] "s"(reg + 4096), [hb2] "s"(hb + 4096)
        : "memory");
  }

  {
    const u32x4 z = {0u, 0u, 0u, 0u};
    *(LAS u32x4a*)(reg + O_SC + lane * 16) = z;
    if (lane < 48) *(LAS u32x4a*)(reg + O_SC + 1024 + lane * 16) = z;
  }

  const unsigned a_row = reg + n * 32 + g * 8;
  const unsigned a_zero = reg + O_SC + 208 * 2;
  const unsigned a_tgt = reg + O_TGT + g * 8;
  const unsigned a_tr = reg + lane * 8;
  const unsigned a_sc = reg + O_SC + (n & 3) * SC_ROW + g * 16;
  const unsigned slot = (lane & 32) + ((lane >> 2) & 3) * 8 + ((lane >> 4) & 1) * 4 + (lane & 3);
  const unsigned a_sw = reg + O_SC + slot * 2;
  const unsigned a_mk = reg + O_MASK + lane * 2;

  asm volatile("s_waitcnt vmcnt(7)\n\ts_barrier" : "+v"(b2raw)::"memory");
  const float cs = -1.4426950408889634f;
  float wac[4], wd[4];
  u32x2 abc;
  {
    const unsigned a_w = sbase + O_W1 + lane * 8;
    u32x2 fa = lds_tr(a_w), fb = lds_tr(a_w + 512), fc = lds_tr(a_w + 1024), fd = lds_tr(a_w + 1536);
    float c0 = bflo(fc[0]), c1 = bfhi(fc[0]), c2 = bflo(fc[1]), c3 = bfhi(fc[1]);
    wac[0] = cs * (bflo(fa[0]) + c0);
    wac[1] = cs * (bfhi(fa[0]) + c1);
    wac[2] = cs * (bflo(fa[1]) + c2);
    wac[3] = cs * (bfhi(fa[1]) + c3);
    wd[0] = cs * bflo(fd[0]);
    wd[1] = cs * bfhi(fd[0]);
    wd[2] = cs * bflo(fd[1]);
    wd[3] = cs * bfhi(fd[1]);
    abc[0] = cvtpk(cs * (bflo(fb[0]) - c0), cs * (bfhi(fb[0]) - c1));
    abc[1] = cvtpk(cs * (bflo(fb[1]) - c2), cs * (bfhi(fb[1]) - c3));
  }
  const float b2f = bflo(b2raw);
  const f32x4 b2v = {b2f, b2f, b2f, b2f};

  VMWAIT(6);
  f32x4 cb1;
  u32x4 a2a, a2b;
  {
    u32x2 fb1 = lds_rd64(reg + O_B1 + g * 8);
    u32x2 fw2 = lds_rd64(reg + O_W2 + g * 8);
    cb1[0] = cs * bflo(fb1[0]);
    cb1[1] = cs * bfhi(fb1[0]);
    cb1[2] = cs * bflo(fb1[1]);
    cb1[3] = cs * bfhi(fb1[1]);
    a2a[0] = n == 0 ? fw2[0] : 0u;
    a2a[1] = n == 0 ? fw2[1] : 0u;
    a2a[2] = n == 4 ? fw2[0] : 0u;
    a2a[3] = n == 4 ? fw2[1] : 0u;
    a2b[0] = n == 8 ? fw2[0] : 0u;
    a2b[1] = n == 8 ? fw2[1] : 0u;
    a2b[2] = n == 12 ? fw2[0] : 0u;
    a2b[3] = n == 12 ? fw2[1] : 0u;
  }
  u32x2 weff[4];
  f32x4 bias[4];
#pragma unroll
  for (int s = 0; s < 4; ++s) {
    u32x2 t = lds_rd64(a_tgt + 32 * s);
    weff[s][0] = cvtpk(fmaf(bflo(t[0]), wd[0], wac[0]), fmaf(bfhi(t[0]), wd[1], wac[1]));
    weff[s][1] = cvtpk(fmaf(bflo(t[1]), wd[2], wac[2]), fmaf(bfhi(t[1]), wd[3], wac[3]));
    bias[s] = mfma16(abc, t, cb1);
  }
  auto tile = [&](int t, int s) -> u32x2 {
    u32x2 h = lds_rd64(a_row + 512 * t);
    return sig4(mfma16(weff[s], h, bias[s]));
  };
  auto tile2 = [&](int t, int s, unsigned cut) -> u32x2 {
    const bool lo = n < cut;
    u32x2 tt = lds_rd64(a_tgt + 32 * s + (lo ? 0u : 32u));
    u32x2 h0 = lds_rd64(lo ? a_row + 512 * t : a_zero);
    u32x2 h1 = lds_rd64(lo ? a_zero : a_row + 512 * t);
    f32x4 d = mfma16(abc, tt, cb1);
    d = mfma16(weff[s], h0, d);
    d = mfma16(weff[s + 1], h1, d);
    return sig4(d);
  };
  auto put = [&](int q, float raw, unsigned cut) {
    unsigned short mk = *(const LAS u16a*)(a_mk + 128 * q);
    float sc = raw * __uint_as_float((unsigned)mk << 16);
    unsigned pk = cvtpk(sc, sc);
    unsigned a = a_sw + 128 * q + (lane < cut ? q * SC_ROW : (q + 1) * SC_ROW);
    if (q == 3) a = lane < cut ? a : reg + O_DUMMY;
    *(LAS u16a*)(a) = (unsigned short)pk;
  };
  f32x4 acc = {0.f, 0.f, 0.f, 0.f};
  auto fin = [&](int q) {
    u32x2 x0 = lds_tr(a_tr + 1024 * q), x1 = lds_tr(a_tr + 1024 * q + 512);
    u32x4 a = {x0[0], x0[1], x1[0], x1[1]};
    acc = mfma32(a, lds_rd128(a_sc + 64 * q), acc);
  };
  auto pair = [&](u32x4 a2, u32x2 s0, u32x2 s1, f32x4 c) -> f32x4 {
    u32x4 b = {s0[0], s0[1], s1[0], s1[1]};
    return mfma32(a2, b, c);
  };

  {
    const unsigned nn = n < 8 ? n : 7;
    u32x2 h = lds_rd64(reg + 6144 + nn * 32 + g * 8);
    u32x2 s12 = sig4(mfma16(weff[3], h, bias[3]));
    u32x2 a2 = {a2a[0], a2a[1]};
    f32x4 r = mfma16(a2, s12, b2v);
    put(3, r[0], 8);
    u32x2 x0 = lds_tr(reg + 6144 + (lane & 31) * 8);
    acc = mfma16(x0, lds_rd64(a_sc + 64 * 6), acc);
  }
  u32x2 s0, s1;
  f32x4 r;
  VMWAIT(5);
  s0 = tile(0, 0);
  s1 = tile(1, 0);
  r = pair(a2a, s0, s1, b2v);
  VMWAIT(4);
  s0 = tile(2, 0);
  s1 = tile2(3, 0, 2);
  r = pair(a2b, s0, s1, r);
  put(0, r[0], 50);
  fin(0);
  fin(1);
  VMWAIT(3);
  s0 = tile(4, 1);
  s1 = tile(5, 1);
  r = pair(a2a, s0, s1, b2v);
  VMWAIT(2);
  s0 = tile2(6, 1, 4);
  s1 = tile(7, 2);
  r = pair(a2b, s0, s1, r);
  put(1, r[0], 36);
  fin(2);
  fin(3);
  VMWAIT(1);
  s0 = tile(8, 2);
  s1 = tile2(9, 2, 6);
  r = pair(a2a, s0, s1, b2v);
  VMWAIT(0);
  s0 = tile(10, 3);
  s1 = tile(11, 3);
  r = pair(a2b, s0, s1, r);
  put(2, r[0], 22);
  fin(4);
  fin(5);
  if (n < 4) {
    u32x2 o = {cvtpk(acc[0], acc[1]), cvtpk(acc[2], acc[3])};
    *(u32x2*)(out + (size_t)gw * 128 + n * 32 + g * 8) = o;
  }
}

extern "C" void kernel_launch(void* const* d_in, const int* in_sizes, int n_in, void* d_out, int out_size, void* d_ws,
                              size_t ws_size, hipStream_t stream) {
  const int B = in_sizes[0] / 16;
  din_kernel<<<B / (4 * WAVES), WAVES * 64, 0, stream>>>((const char*)d_in[0], (const char*)d_in[1], (const char*)d_in[2],
                                                        (const char*)d_in[3], (const char*)d_in[4], (const char*)d_in[5],
                                                        (const char*)d_in[6], (char*)d_out);
}
```

```cpp
#include <hip/hip_runtime.h>

#define LAS __attribute__((address_space(3)))

__global__ __launch_bounds__(1024, 4) __attribute__((aligned(4096))) void din_kernel(const void* target, const void* hist, const void* mask,
                                                      const void* w1, const void* b1, const void* w2,
                                                      const void* b2, void* out) {
  __shared__ __attribute__((aligned(16))) char smem[119040];
  const unsigned sb = (unsigned)(size_t)(LAS char*)smem;
  unsigned tid = threadIdx.x;
  asm volatile(
      "s_load_dwordx16 s[16:31], %[ka], 0x0\n"
      "v_readfirstlane_b32 s34, %[tid]\n"
      "s_setprio 3\n"
      "v_and_b32 v46, 63, %[tid]\n"
      "s_lshr_b32 s34, s34, 6\n"
      "s_mul_i32 s35, %[wg], 16\n"
      "s_add_u32 s35, s35, s34\n"
      "s_mul_i32 s36, s34, 6928\n"
      "s_add_u32 s36, s36, %[sb]\n"
      "s_add_u32 s36, s36, 8192\n"
      "v_lshlrev_b32 v53, 4, v46\n"
      "s_mul_i32 s37, s35, 6400\n"
      "v_lshrrev_b32 v54, 5, v46\n"
      "v_add_u32 v49, s37, v53\n"
      "s_movk_i32 s46, 1152\n"
      "v_mad_u32_u24 %[tid], v54, s46, v49\n"
      "v_and_b32 v47, 15, v46\n"
      "v_lshrrev_b32 v48, 4, v46\n"
      "v_and_b32 v52, 16, v46\n"
      "v_lshlrev_b32 v1, 5, v47\n"
      "s_lshl_b32 s46, 512, s34\n"
      "s_sub_u32 s46, s46, 512\n"
      "s_max_u32 s38, s46, 1024\n"
      "v_add3_u32 v52, v52, v1, s46\n"
      "s_sub_u32 s38, s38, s46\n"
      "v_mad_u32_u24 v52, v54, s38, v52\n"
      "v_lshrrev_b32 v54, 3, v46\n"
      "s_movk_i32 s46, 3072\n"
      "v_mad_u32_u24 v49, v54, s46, v49\n"
      "s_mul_i32 s37, s35, 400\n"
      "v_add_u32 v50, s37, v53\n"
      "s_lshl_b32 s37, s35, 7\n"
      "v_add_u32 v51, s37, v53\n"
      "v_lshlrev_b32 v4, 3, v48\n"
      "v_mov_b32 v10, 0\n"
      "s_add_u32 m0, s36, 6800\n"
      "s_cmp_gt_u32 s34, 2\n"
      "s_waitcnt lgkmcnt(0)\n"
      "s_cbranch_scc1 1f\n"
      "global_load_dwordx4 v[102:105], v52, s[22:23]\n"
      "1:\n"
      "s_cmp_lg_u32 s34, 3\n"
      "s_cbranch_scc1 2f\n"
      "global_load_dwordx2 v[102:103], v4, s[24:25]\n"
      "global_load_dwordx2 v[104:105], v4, s[26:27]\n"
      "global_load_ushort v110, v10, s[28:29]\n"
      "2:\n"
      "s_mov_b64 exec, 0xff\n"
      "global_load_lds_dwordx4 v51, s[16:17] nt\n"
      "s_add_u32 m0, s36, 4608\n"
      "s_mov_b64 exec, 0xffff\n"
      "global_load_lds_dwordx4 v49, s[18:19] offset:1536 nt\n"
      "s_add_u32 m0, s36, 6400\n"
      "s_mov_b64 exec, 0x1ffffff\n"
      "global_load_lds_dwordx4 v50, s[20:21] nt\n"
      "s_mov_b64 exec, -1\n"
      "s_add_u32 s42, s18, 3200\n"
      "s_addc_u32 s43, s19, 0\n"
      "s_add_u32 m0, s36, 0\n"
      "s_nop 0\n"
      "global_load_lds_dwordx4 %[tid], s[18:19] nt\n"
      "s_add_u32 m0, s36, 1024\n"
      "s_nop 0\n"
      "global_load_lds_dwordx4 %[tid], s[42:43] nt\n"
      "v_lshlrev_b32 v9, 1, v46\n"
      "v_add_u32 v9, s37, v9\n"
      "v_add_u32 v8, %[sb], v53\n"
      "v_add_u32 v4, s36, v4\n"
      "v_add_u32 v1, v1, v4\n"
      "v_lshlrev_b32 v2, 3, v46\n"
      "v_and_b32 v3, 0xff, v2\n"
      "v_add_u32 v2, s36, v2\n"
      "v_add_u32 v3, s36, v3\n"
      "v_bfe_u32 v53, v46, 1, 2\n"
      "v_lshl_add_u32 v5, v53, 5, v4\n"
      "v_lshrrev_b32 v53, 2, v47\n"
      "s_movk_i32 s46, 100\n"
      "v_mad_u32_u24 v6, v53, s46, v4\n"
      "v_and_b32 v53, 4, v47\n"
      "v_add_u32 v6, v6, v53\n"
      "s_movk_i32 s46, 200\n"
      "v_mul_u32_u24 v7, s46, v48\n"
      "v_add_u32 v7, s36, v7\n"
      "v_and_b32 v53, 1, v46\n"
      "v_lshlrev_b32 v53, 4, v53\n"
      "v_mov_b32 v54, 0xffff\n"
      "v_lshlrev_b32 v54, v53, v54\n"
      "v_mov_b32 v11, 0\n"
      "s_mov_b32 exec_lo, 0x300003\n"
      "s_mov_b32 exec_hi, 0x30000300\n"
      "v_mov_b32 v10, v54\n"
      "s_lshl_b64 exec, exec, 2\n"
      "v_mov_b32 v11, v54\n"
      "s_mov_b64 exec, -1\n"
      "v_mov_b32 v62, 0\n"
      "v_mov_b32 v63, 0\n"
      "v_mov_b32 v64, 0\n"
      "v_mov_b32 v65, 0\n"
      "v_mov_b32 v66, 0\n"
      "v_mov_b32 v67, 0\n"
      "v_mov_b32 v68, 0\n"
      "v_mov_b32 v69, 0\n"
      "v_mov_b32 v70, 0\n"
      "v_mov_b32 v71, 0\n"
      "v_mov_b32 v72, 0\n"
      "v_mov_b32 v73, 0\n"
      "v_mov_b32 v74, 0\n"
      "v_mov_b32 v75, 0\n"
      "v_mov_b32 v76, 0\n"
      "v_mov_b32 v77, 0\n"
      "v_mov_b32 v86, 0\n"
      "v_mov_b32 v87, 0\n"
      "v_mov_b32 v88, 0\n"
      "v_mov_b32 v89, 0\n"
      "v_mov_b32 v90, 0\n"
      "v_mov_b32 v91, 0\n"
      "v_mov_b32 v92, 0\n"
      "v_mov_b32 v93, 0\n"
      "s_cmp_gt_u32 s34, 3\n"
      "s_cbranch_scc1 10f\n"
      "s_mov_b32 s38, 0xbfb8aa3b\n"
      "s_mov_b32 s39, s38\n"
      "s_cmp_eq_u32 s34, 3\n"
      "s_cbranch_scc1 3f\n"
      "s_lshl_b32 s46, s34, 3\n"
      "s_lshr_b32 s44, 0xbf803f80, s46\n"
      "s_lshr_b32 s44, s44, s46\n"
      "s_and_b32 s44, s44, 0xffff\n"
      "v_mov_b32 v54, s44\n"
      "s_mov_b32 exec_hi, 0\n"
      "v_mov_b32 v54, 0x3f80\n"
      "s_mov_b32 exec_hi, -1\n"
      "v_lshlrev_b32 v54, v53, v54\n"
      "v_mov_b32 v114, 0\n"
      "v_mov_b32 v115, 0\n"
      "v_mov_b32 v116, 0\n"
      "v_mov_b32 v117, 0\n"
      "s_mov_b32 exec_lo, 0x03000003\n"
      "s_mov_b32 exec_hi, 0x03000003\n"
      "v_mov_b32 v114, v54\n"
      "s_lshl_b64 exec, exec, 2\n"
      "v_mov_b32 v115, v54\n"
      "s_lshl_b64 exec, exec, 2\n"
      "v_mov_b32 v116, v54\n"
      "s_lshl_b64 exec, exec, 2\n"
      "v_mov_b32 v117, v54\n"
      "s_mov_b64 exec, -1\n"
      "s_lshl_b32 s46, s34, 10\n"
      "v_add_u32 v55, s46, v8\n"
      "s_cmp_eq_u32 s34, 0\n"
      "s_waitcnt vmcnt(5)\n"
      "v_mfma_f32_16x16x32_bf16 v[106:109], v[102:105], v[114:117], 0\n"
      "s_nop 7\n"
      "v_pk_mul_f32 v[106:107], v[106:107], s[38:39]\n"
      "v_pk_mul_f32 v[108:109], v[108:109], s[38:39]\n"
      "s_cbranch_scc1 4f\n"
      "v_cvt_pk_bf16_f32 v106, v106, v107\n"
      "v_cvt_pk_bf16_f32 v107, v108, v109\n"
      "4:\n"
      "ds_write_b128 v55, v[106:109] offset:0\n"
      "s_branch 10f\n"
      "3:\n"
      "ds_write_b128 v8, v[62:65] offset:4096\n"
      "ds_write_b128 v8, v[62:65] offset:5120\n"
      "s_waitcnt vmcnt(5)\n"
      "v_lshlrev_b32 v106, 16, v102\n"
      "v_and_b32 v107, 0xffff0000, v102\n"
      "v_lshlrev_b32 v108, 16, v103\n"
      "v_and_b32 v109, 0xffff0000, v103\n"
      "v_pk_mul_f32 v[106:107], v[106:107], s[38:39]\n"
      "v_pk_mul_f32 v[108:109], v[108:109], s[38:39]\n"
      "ds_write_b128 v8, v[106:109] offset:3072\n"
      "ds_write_b64 v8, v[104:105] offset:7168\n"
      "v_lshlrev_b32 v110, 16, v110\n"
      "v_mov_b32 v111, v110\n"
      "v_mov_b32 v112, v110\n"
      "v_mov_b32 v113, v110\n"
      "ds_write_b128 v8, v[110:113] offset:6144\n"
      "s_mov_b32 exec_lo, 0x00010001\n"
      "s_mov_b32 exec_hi, 0x00010001\n"
      "ds_write_b64 v8, v[104:105] offset:4096\n"
      "s_lshl_b64 exec, exec, 4\n"
      "ds_write_b64 v8, v[104:105] offset:4104\n"
      "s_lshl_b64 exec, exec, 4\n"
      "ds_write_b64 v8, v[104:105] offset:5120\n"
      "s_lshl_b64 exec, exec, 4\n"
      "ds_write_b64 v8, v[104:105] offset:5128\n"
      "s_mov_b64 exec, -1\n"
      "10:\n"
      "s_waitcnt lgkmcnt(0)\n"
      "s_barrier\n"
      "s_nop 1\n"
      "ds_read_b128 v[12:15], v8 offset:0\n"
      "ds_read_b64 v[18:19], v8 offset:2048\n"
      "ds_read_b64 v[16:17], v8 offset:1024\n"
      "ds_read_b128 v[20:23], v8 offset:3072\n"
      "s_add_u32 m0, s36, 1536\n"
      "s_nop 0\n"
      "global_load_lds_dwordx4 %[tid], s[18:19] offset:512 nt\n"
      "s_add_u32 m0, s36, 2560\n"
      "s_nop 0\n"
      "global_load_lds_dwordx4 %[tid], s[42:43] offset:512 nt\n"
      "s_setprio 3\n"
      "s_waitcnt vmcnt(6)\n"
      "ds_read_b64 v[94:95], v4 offset:6800\n"
      "ds_read_b64 v[96:97], v4 offset:6832\n"
      "ds_read_b64 v[98:99], v4 offset:6864\n"
      "ds_read_b64 v[100:101], v4 offset:6896\n"
      "ds_read_b64 v[126:127], v5 offset:6800\n"
      "ds_read_b128 v[32:35], v8 offset:6144\n"
      "ds_read_b64 v[36:37], v8 offset:7168\n"
      "ds_read_b128 v[24:27], v8 offset:4096\n"
      "ds_read_b128 v[28:31], v8 offset:5120\n"
      "s_waitcnt lgkmcnt(4)\n"
      "v_and_b32 v78, v94, v10\n"
      "v_and_b32 v79, v95, v11\n"
      "v_and_b32 v80, v96, v10\n"
      "v_and_b32 v81, v97, v11\n"
      "v_and_b32 v82, v98, v10\n"
      "v_and_b32 v83, v99, v11\n"
      "v_and_b32 v84, v100, v10\n"
      "v_and_b32 v85, v101, v11\n"
      "v_mfma_f32_16x16x16_bf16 v[102:105], v[78:79], v[18:19], v[12:15]\n"
      "v_mfma_f32_16x16x16_bf16 v[106:109], v[80:81], v[18:19], v[12:15]\n"
      "v_mfma_f32_16x16x16_bf16 v[110:113], v[82:83], v[18:19], v[12:15]\n"
      "v_mfma_f32_16x16x16_bf16 v[114:117], v[84:85], v[18:19], v[12:15]\n"
      "v_mfma_f32_16x16x16_bf16 v[122:125], v[16:17], v[126:127], v[20:23]\n"
      "s_waitcnt vmcnt(5)\n"
      "s_mov_b32 exec_lo, 0x00030003\n"
      "s_mov_b32 exec_hi, 0x00030003\n"
      "ds_read_b64 v[86:87], v1 offset:6144\n"
      "s_lshl_b64 exec, exec, 2\n"
      "ds_read_b64 v[88:89], v1 offset:6144\n"
      "s_lshl_b64 exec, exec, 2\n"
      "ds_read_b64 v[90:91], v1 offset:6144\n"
      "s_lshl_b64 exec, exec, 2\n"
      "ds_read_b64 v[92:93], v1 offset:6144\n"
      "s_mov_b64 exec, -1\n"
      "v_mfma_f32_16x16x16_bf16 v[46:49], v[16:17], v[94:95], v[20:23]\n"
      "v_mfma_f32_16x16x16_bf16 v[50:53], v[16:17], v[96:97], v[20:23]\n"
      "v_mfma_f32_16x16x16_bf16 v[54:57], v[16:17], v[98:99], v[20:23]\n"
      "v_mfma_f32_16x16x16_bf16 v[58:61], v[16:17], v[100:101], v[20:23]\n"
      "v_cvt_pk_bf16_f32 v38, v102, v103\n"
      "v_cvt_pk_bf16_f32 v39, v104, v105\n"
      "v_cvt_pk_bf16_f32 v40, v106, v107\n"
      "v_cvt_pk_bf16_f32 v41, v108, v109\n"
      "v_cvt_pk_bf16_f32 v42, v110, v111\n"
      "v_cvt_pk_bf16_f32 v43, v112, v113\n"
      "v_cvt_pk_bf16_f32 v44, v114, v115\n"
      "v_cvt_pk_bf16_f32 v45, v116, v117\n"
      "s_waitcnt lgkmcnt(0)\n"
      "v_mfma_f32_16x16x32_bf16 v[122:125], v[38:41], v[86:89], v[122:125]\n"
      "v_mfma_f32_16x16x32_bf16 v[122:125], v[42:45], v[90:93], v[122:125]\n"
      "ds_read_b64_tr_b16 v[102:103], v3 offset:6144\n"
      "ds_read_b64_tr_b16 v[106:107], v3 offset:6144\n"
      "ds_read_b64_tr_b16 v[110:111], v3 offset:6144\n"
      "ds_read_b64_tr_b16 v[114:115], v3 offset:6144\n"
      "s_waitcnt vmcnt(4)\n"
      "s_mov_b64 exec, 0x1000001\n"
      "ds_read_u16_d16_hi v62, v7 offset:6496\n"
      "ds_read_u16_d16_hi v63, v7 offset:6498\n"
      "s_mov_b64 exec, 0x10000010\n"
      "ds_read_u16_d16_hi v64, v7 offset:6500\n"
      "ds_read_u16_d16_hi v65, v7 offset:6502\n"
      "s_mov_b64 exec, -1\n"
      "ds_read_u16_d16_hi v66, v6 offset:6400\n"
      "ds_read_u16_d16_hi v67, v6 offset:6402\n"
      "ds_read_u16_d16_hi v68, v6 offset:6404\n"
      "ds_read_u16_d16_hi v69, v6 offset:6406\n"
      "ds_read_u16_d16_hi v70, v6 offset:6432\n"
      "ds_read_u16_d16_hi v71, v6 offset:6434\n"
      "ds_read_u16_d16_hi v72, v6 offset:6436\n"
      "ds_read_u16_d16_hi v73, v6 offset:6438\n"
      "ds_read_u16_d16_hi v74, v6 offset:6464\n"
      "ds_read_u16_d16_hi v75, v6 offset:6466\n"
      "ds_read_u16_d16_hi v76, v6 offset:6468\n"
      "ds_read_u16_d16_hi v77, v6 offset:6470\n"
      "v_exp_f32 v122, v122\n"
      "v_exp_f32 v123, v123\n"
      "v_exp_f32 v124, v124\n"
      "v_exp_f32 v125, v125\n"
      "v_pk_add_f32 v[122:123], v[122:123], 1.0 op_sel_hi:[1,0]\n"
      "v_pk_add_f32 v[124:125], v[124:125], 1.0 op_sel_hi:[1,0]\n"
      "v_rcp_f32 v122, v122\n"
      "v_rcp_f32 v123, v123\n"
      "v_rcp_f32 v124, v124\n"
      "v_rcp_f32 v125, v125\n"
      "v_cvt_pk_bf16_f32 v118, v122, v123\n"
      "v_cvt_pk_bf16_f32 v119, v124, v125\n"
      "s_nop 1\n"
      "v_mfma_f32_16x16x16_bf16 v[122:125], v[118:119], v[36:37], v[32:35]\n"
      "s_waitcnt lgkmcnt(0)\n"
      "s_nop 6\n"
      "v_pk_mul_f32 v[122:123], v[122:123], v[62:63]\n"
      "v_pk_mul_f32 v[124:125], v[124:125], v[64:65]\n"
      "v_cvt_pk_bf16_f32 v78, v122, v123\n"
      "v_cvt_pk_bf16_f32 v79, v124, v125\n"
      "s_waitcnt vmcnt(3)\n"
      "ds_read_b64 v[20:21], v1 offset:0\n"
      "ds_read_b64 v[22:23], v1 offset:512\n"
      "ds_read_b64_tr_b16 v[104:105], v2 offset:0\n"
      "ds_read_b64_tr_b16 v[108:109], v2 offset:512\n"
      "s_add_u32 m0, s36, 3072\n"
      "s_nop 0\n"
      "global_load_lds_dwordx4 %[tid], s[18:19] offset:1024 nt\n"
      "s_waitcnt lgkmcnt(3)\n"
      "v_mfma_f32_16x16x16_bf16 v[12:15], v[38:39], v[20:21], v[46:49]\n"
      "s_waitcnt lgkmcnt(2)\n"
      "v_mfma_f32_16x16x16_bf16 v[16:19], v[40:41], v[22:23], v[50:53]\n"
      "s_nop 5\n"
      "v_exp_f32 v12, v12\n"
      "v_exp_f32 v13, v13\n"
      "v_exp_f32 v14, v14\n"
      "v_exp_f32 v15, v15\n"
      "v_exp_f32 v16, v16\n"
      "v_exp_f32 v17, v17\n"
      "v_exp_f32 v18, v18\n"
      "v_exp_f32 v19, v19\n"
      "v_pk_add_f32 v[12:13], v[12:13], 1.0 op_sel_hi:[1,0]\n"
      "v_pk_add_f32 v[14:15], v[14:15], 1.0 op_sel_hi:[1,0]\n"
      "v_pk_add_f32 v[16:17], v[16:17], 1.0 op_sel_hi:[1,0]\n"
      "v_pk_add_f32 v[18:19], v[18:19], 1.0 op_sel_hi:[1,0]\n"
      "v_rcp_f32 v12, v12\n"
      "v_rcp_f32 v13, v13\n"
      "v_rcp_f32 v14, v14\n"
      "v_rcp_f32 v15, v15\n"
      "v_rcp_f32 v16, v16\n"
      "v_rcp_f32 v17, v17\n"
      "v_rcp_f32 v18, v18\n"
      "v_rcp_f32 v19, v19\n"
      "v_cvt_pk_bf16_f32 v118, v12, v13\n"
      "v_cvt_pk_bf16_f32 v119, v14, v15\n"
      "v_cvt_pk_bf16_f32 v120, v16, v17\n"
      "v_cvt_pk_bf16_f32 v121, v18, v19\n"
      "s_nop 1\n"
      "v_mfma_f32_16x16x32_bf16 v[122:125], v[118:121], v[24:27], v[32:35]\n"
      "s_setprio 2\n"
      "s_waitcnt vmcnt(3)\n"
      "ds_read_b64 v[20:21], v1 offset:1024\n"
      "ds_read_b64 v[22:23], v1 offset:1536\n"
      "ds_read_b64_tr_b16 v[112:113], v2 offset:1024\n"
      "ds_read_b64_tr_b16 v[116:117], v2 offset:1536\n"
      "s_add_u32 m0, s36, 4096\n"
      "s_nop 0\n"
      "global_load_lds_dwordx4 %[tid], s[42:43] offset:1024 nt\n"
      "s_waitcnt lgkmcnt(3)\n"
      "v_mfma_f32_16x16x16_bf16 v[12:15], v[42:43], v[20:21], v[54:57]\n"
      "s_waitcnt lgkmcnt(2)\n"
      "v_mfma_f32_16x16x16_bf16 v[16:19], v[44:45], v[22:23], v[58:61]\n"
      "s_nop 5\n"
      "v_exp_f32 v12, v12\n"
      "v_exp_f32 v13, v13\n"
      "v_exp_f32 v14, v14\n"
      "v_exp_f32 v15, v15\n"
      "v_exp_f32 v16, v16\n"
      "v_exp_f32 v17, v17\n"
      "v_exp_f32 v18, v18\n"
      "v_exp_f32 v19, v19\n"
      "v_pk_add_f32 v[12:13], v[12:13], 1.0 op_sel_hi:[1,0]\n"
      "v_pk_add_f32 v[14:15], v[14:15], 1.0 op_sel_hi:[1,0]\n"
      "v_pk_add_f32 v[16:17], v[16:17], 1.0 op_sel_hi:[1,0]\n"
      "v_pk_add_f32 v[18:19], v[18:19], 1.0 op_sel_hi:[1,0]\n"
      "v_rcp_f32 v12, v12\n"
      "v_rcp_f32 v13, v13\n"
      "v_rcp_f32 v14, v14\n"
      "v_rcp_f32 v15, v15\n"
      "v_rcp_f32 v16, v16\n"
      "v_rcp_f32 v17, v17\n"
      "v_rcp_f32 v18, v18\n"
      "v_rcp_f32 v19, v19\n"
      "v_cvt_pk_bf16_f32 v118, v12, v13\n"
      "v_cvt_pk_bf16_f32 v119, v14, v15\n"
      "v_cvt_pk_bf16_f32 v120, v16, v17\n"
      "v_cvt_pk_bf16_f32 v121, v18, v19\n"
      "s_nop 1\n"
      "v_mfma_f32_16x16x32_bf16 v[122:125], v[118:121], v[28:31], v[122:125]\n"
      "s_nop 7\n"
      "v_pk_mul_f32 v[122:123], v[122:123], v[66:67]\n"
      "v_pk_mul_f32 v[124:125], v[124:125], v[68:69]\n"
      "v_cvt_pk_bf16_f32 v80, v122, v123\n"
      "v_cvt_pk_bf16_f32 v81, v124, v125\n"
      "s_waitcnt lgkmcnt(0)\n"
      "s_nop 0\n"
      "v_mfma_f32_16x16x32_bf16 v[86:89], v[78:81], v[102:105], 0\n"
      "v_mfma_f32_16x16x32_bf16 v[90:93], v[78:81], v[106:109], 0\n"
      "v_mfma_f32_16x16x32_bf16 v[94:97], v[78:81], v[110:113], 0\n"
      "v_mfma_f32_16x16x32_bf16 v[98:101], v[78:81], v[114:117], 0\n"
      "s_waitcnt vmcnt(3)\n"
      "ds_read_b64 v[20:21], v1 offset:2048\n"
      "ds_read_b64 v[22:23], v1 offset:2560\n"
      "ds_read_b64_tr_b16 v[102:103], v2 offset:2048\n"
      "ds_read_b64_tr_b16 v[106:107], v2 offset:2560\n"
      "s_waitcnt lgkmcnt(3)\n"
      "v_mfma_f32_16x16x16_bf16 v[12:15], v[38:39], v[20:21], v[46:49]\n"
      "s_waitcnt lgkmcnt(2)\n"
      "v_mfma_f32_16x16x16_bf16 v[16:19], v[40:41], v[22:23], v[50:53]\n"
      "s_nop 5\n"
      "v_exp_f32 v12, v12\n"
      "v_exp_f32 v13, v13\n"
      "v_exp_f32 v14, v14\n"
      "v_exp_f32 v15, v15\n"
      "v_exp_f32 v16, v16\n"
      "v_exp_f32 v17, v17\n"
      "v_exp_f32 v18, v18\n"
      "v_exp_f32 v19, v19\n"
      "v_pk_add_f32 v[12:13], v[12:13], 1.0 op_sel_hi:[1,0]\n"
      "v_pk_add_f32 v[14:15], v[14:15], 1.0 op_sel_hi:[1,0]\n"
      "v_pk_add_f32 v[16:17], v[16:17], 1.0 op_sel_hi:[1,0]\n"
      "v_pk_add_f32 v[18:19], v[18:19], 1.0 op_sel_hi:[1,0]\n"
      "v_rcp_f32 v12, v12\n"
      "v_rcp_f32 v13, v13\n"
      "v_rcp_f32 v14, v14\n"
      "v_rcp_f32 v15, v15\n"
      "v_rcp_f32 v16, v16\n"
      "v_rcp_f32 v17, v17\n"
      "v_rcp_f32 v18, v18\n"
      "v_rcp_f32 v19, v19\n"
      "v_cvt_pk_bf16_f32 v118, v12, v13\n"
      "v_cvt_pk_bf16_f32 v119, v14, v15\n"
      "v_cvt_pk_bf16_f32 v120, v16, v17\n"
      "v_cvt_pk_bf16_f32 v121, v18, v19\n"
      "s_nop 1\n"
      "v_mfma_f32_16x16x32_bf16 v[122:125], v[118:121], v[24:27], v[32:35]\n"
      "s_setprio 1\n"
      "s_waitcnt vmcnt(2)\n"
      "ds_read_b64 v[20:21], v1 offset:3072\n"
      "ds_read_b64 v[22:23], v1 offset:3584\n"
      "ds_read_b64_tr_b16 v[110:111], v2 offset:3072\n"
      "ds_read_b64_tr_b16 v[114:115], v2 offset:3584\n"
      "s_waitcnt lgkmcnt(3)\n"
      "v_mfma_f32_16x16x16_bf16 v[12:15], v[42:43], v[20:21], v[54:57]\n"
      "s_waitcnt lgkmcnt(2)\n"
      "v_mfma_f32_16x16x16_bf16 v[16:19], v[44:45], v[22:23], v[58:61]\n"
      "s_nop 5\n"
      "v_exp_f32 v12, v12\n"
      "v_exp_f32 v13, v13\n"
      "v_exp_f32 v14, v14\n"
      "v_exp_f32 v15, v15\n"
      "v_exp_f32 v16, v16\n"
      "v_exp_f32 v17, v17\n"
      "v_exp_f32 v18, v18\n"
      "v_exp_f32 v19, v19\n"
      "v_pk_add_f32 v[12:13], v[12:13], 1.0 op_sel_hi:[1,0]\n"
      "v_pk_add_f32 v[14:15], v[14:15], 1.0 op_sel_hi:[1,0]\n"
      "v_pk_add_f32 v[16:17], v[16:17], 1.0 op_sel_hi:[1,0]\n"
      "v_pk_add_f32 v[18:19], v[18:19], 1.0 op_sel_hi:[1,0]\n"
      "v_rcp_f32 v12, v12\n"
      "v_rcp_f32 v13, v13\n"
      "v_rcp_f32 v14, v14\n"
      "v_rcp_f32 v15, v15\n"
      "v_rcp_f32 v16, v16\n"
      "v_rcp_f32 v17, v17\n"
      "v_rcp_f32 v18, v18\n"
      "v_rcp_f32 v19, v19\n"
      "v_cvt_pk_bf16_f32 v118, v12, v13\n"
      "v_cvt_pk_bf16_f32 v119, v14, v15\n"
      "v_cvt_pk_bf16_f32 v120, v16, v17\n"
      "v_cvt_pk_bf16_f32 v121, v18, v19\n"
      "s_nop 1\n"
      "v_mfma_f32_16x16x32_bf16 v[122:125], v[118:121], v[28:31], v[122:125]\n"
      "s_nop 7\n"
      "v_pk_mul_f32 v[122:123], v[122:123], v[70:71]\n"
      "v_pk_mul_f32 v[124:125], v[124:125], v[72:73]\n"
      "v_cvt_pk_bf16_f32 v82, v122, v123\n"
      "v_cvt_pk_bf16_f32 v83, v124, v125\n"
      "s_waitcnt vmcnt(1)\n"
      "ds_read_b64 v[20:21], v1 offset:4096\n"
      "ds_read_b64 v[22:23], v1 offset:4608\n"
      "ds_read_b64_tr_b16 v[104:105], v2 offset:4096\n"
      "ds_read_b64_tr_b16 v[108:109], v2 offset:4608\n"
      "s_waitcnt lgkmcnt(3)\n"
      "v_mfma_f32_16x16x16_bf16 v[12:15], v[38:39], v[20:21], v[46:49]\n"
      "s_waitcnt lgkmcnt(2)\n"
      "v_mfma_f32_16x16x16_bf16 v[16:19], v[40:41], v[22:23], v[50:53]\n"
      "s_nop 5\n"
      "v_exp_f32 v12, v12\n"
      "v_exp_f32 v13, v13\n"
      "v_exp_f32 v14, v14\n"
      "v_exp_f32 v15, v15\n"
      "v_exp_f32 v16, v16\n"
      "v_exp_f32 v17, v17\n"
      "v_exp_f32 v18, v18\n"
      "v_exp_f32 v19, v19\n"
      "v_pk_add_f32 v[12:13], v[12:13], 1.0 op_sel_hi:[1,0]\n"
      "v_pk_add_f32 v[14:15], v[14:15], 1.0 op_sel_hi:[1,0]\n"
      "v_pk_add_f32 v[16:17], v[16:17], 1.0 op_sel_hi:[1,0]\n"
      "v_pk_add_f32 v[18:19], v[18:19], 1.0 op_sel_hi:[1,0]\n"
      "v_rcp_f32 v12, v12\n"
      "v_rcp_f32 v13, v13\n"
      "v_rcp_f32 v14, v14\n"
      "v_rcp_f32 v15, v15\n"
      "v_rcp_f32 v16, v16\n"
      "v_rcp_f32 v17, v17\n"
      "v_rcp_f32 v18, v18\n"
      "v_rcp_f32 v19, v19\n"
      "v_cvt_pk_bf16_f32 v118, v12, v13\n"
      "v_cvt_pk_bf16_f32 v119, v14, v15\n"
      "v_cvt_pk_bf16_f32 v120, v16, v17\n"
      "v_cvt_pk_bf16_f32 v121, v18, v19\n"
      "s_nop 1\n"
      "v_mfma_f32_16x16x32_bf16 v[122:125], v[118:121], v[24:27], v[32:35]\n"
      "s_setprio 0\n"
      "s_waitcnt vmcnt(0)\n"
      "ds_read_b64 v[20:21], v1 offset:5120\n"
      "ds_read_b64 v[22:23], v1 offset:5632\n"
      "ds_read_b64_tr_b16 v[112:113], v2 offset:5120\n"
      "ds_read_b64_tr_b16 v[116:117], v2 offset:5632\n"
      "s_waitcnt lgkmcnt(3)\n"
      "v_mfma_f32_16x16x16_bf16 v[12:15], v[42:43], v[20:21], v[54:57]\n"
      "s_waitcnt lgkmcnt(2)\n"
      "v_mfma_f32_16x16x16_bf16 v[16:19], v[44:45], v[22:23], v[58:61]\n"
      "s_nop 5\n"
      "v_exp_f32 v12, v12\n"
      "v_exp_f32 v13, v13\n"
      "v_exp_f32 v14, v14\n"
      "v_exp_f32 v15, v15\n"
      "v_exp_f32 v16, v16\n"
      "v_exp_f32 v17, v17\n"
      "v_exp_f32 v18, v18\n"
      "v_exp_f32 v19, v19\n"
      "v_pk_add_f32 v[12:13], v[12:13], 1.0 op_sel_hi:[1,0]\n"
      "v_pk_add_f32 v[14:15], v[14:15], 1.0 op_sel_hi:[1,0]\n"
      "v_pk_add_f32 v[16:17], v[16:17], 1.0 op_sel_hi:[1,0]\n"
      "v_pk_add_f32 v[18:19], v[18:19], 1.0 op_sel_hi:[1,0]\n"
      "v_rcp_f32 v12, v12\n"
      "v_rcp_f32 v13, v13\n"
      "v_rcp_f32 v14, v14\n"
      "v_rcp_f32 v15, v15\n"
      "v_rcp_f32 v16, v16\n"
      "v_rcp_f32 v17, v17\n"
      "v_rcp_f32 v18, v18\n"
      "v_rcp_f32 v19, v19\n"
      "v_cvt_pk_bf16_f32 v118, v12, v13\n"
      "v_cvt_pk_bf16_f32 v119, v14, v15\n"
      "v_cvt_pk_bf16_f32 v120, v16, v17\n"
      "v_cvt_pk_bf16_f32 v121, v18, v19\n"
      "s_nop 1\n"
      "v_mfma_f32_16x16x32_bf16 v[122:125], v[118:121], v[28:31], v[122:125]\n"
      "s_nop 7\n"
      "v_pk_mul_f32 v[122:123], v[122:123], v[74:75]\n"
      "v_pk_mul_f32 v[124:125], v[124:125], v[76:77]\n"
      "v_cvt_pk_bf16_f32 v84, v122, v123\n"
      "v_cvt_pk_bf16_f32 v85, v124, v125\n"
      "s_waitcnt lgkmcnt(0)\n"
      "s_nop 0\n"
      "v_mfma_f32_16x16x32_bf16 v[86:89], v[82:85], v[102:105], v[86:89]\n"
      "v_mfma_f32_16x16x32_bf16 v[90:93], v[82:85], v[106:109], v[90:93]\n"
      "v_mfma_f32_16x16x32_bf16 v[94:97], v[82:85], v[110:113], v[94:97]\n"
      "v_mfma_f32_16x16x32_bf16 v[98:101], v[82:85], v[114:117], v[98:101]\n"
      "s_mov_b64 exec, 0xffff\n"
      "s_nop 3\n"
      "v_cvt_pk_bf16_f32 v118, v86, v86\n"
      "s_lshl_b64 exec, exec, 16\n"
      "v_cvt_pk_bf16_f32 v118, v90, v90\n"
      "s_lshl_b64 exec, exec, 16\n"
      "v_cvt_pk_bf16_f32 v118, v94, v94\n"
      "s_lshl_b64 exec, exec, 16\n"
      "v_cvt_pk_bf16_f32 v118, v98, v98\n"
      "s_mov_b64 exec, -1\n"
      "global_store_short v9, v118, s[30:31]\n"
      : [tid] "+v"(tid)
      : [ka] "s"(__builtin_amdgcn_kernarg_segment_ptr()), [wg] "s"(blockIdx.x), [sb] "s"(sb)
      : "memory", "scc", "vcc", "v1", "v2", "v3", "v4", "v5", "v6", "v7", "v8", "v9", "v10", "v11", "v12", "v13", "v14", "v15", "v16", "v17", "v18", "v19", "v20", "v21", "v22", "v23", "v24", "v25", "v26", "v27", "v28", "v29", "v30", "v31", "v32", "v33", "v34", "v35", "v36", "v37", "v38", "v39", "v40", "v41", "v42", "v43", "v44", "v45", "v46", "v47", "v48", "v49", "v50", "v51", "v52", "v53", "v54", "v55", "v56", "v57", "v58", "v59", "v60", "v61", "v62", "v63", "v64", "v65", "v66", "v67", "v68", "v69", "v70", "v71", "v72", "v73", "v74", "v75", "v76", "v77", "v78", "v79", "v80", "v81", "v82", "v83", "v84", "v85", "v86", "v87", "v88", "v89", "v90", "v91", "v92", "v93", "v94", "v95", "v96", "v97", "v98", "v99", "v100", "v101", "v102", "v103", "v104", "v105", "v106", "v107", "v108", "v109", "v110", "v111", "v112", "v113", "v114", "v115", "v116", "v117", "v118", "v119", "v120", "v121", "v122", "v123", "v124", "v125", "v126", "v127", "s16", "s17", "s18", "s19", "s20", "s21", "s22", "s23", "s24", "s25", "s26", "s27", "s28", "s29", "s30", "s31", "s34", "s35", "s36", "s37", "s38", "s39", "s40", "s41", "s42", "s43", "s44", "s45", "s46");
}

extern "C" void kernel_launch(void* const* d_in, const int* in_sizes, int n_in, void* d_out, int out_size, void* d_ws,
                              size_t ws_size, hipStream_t stream) {
  const int B = in_sizes[0] / 16;
  din_kernel<<<B / (4 * 16), 1024, 0, stream>>>(d_in[0], d_in[1], d_in[2], d_in[3], d_in[4], d_in[5], d_in[6], d_out);
}
```
